```python
import torch

def _softmax(x, axis=-1):
    return torch.softmax(x, dim=axis)
def _gelu(x):
    return torch.nn.functional.gelu(x, approximate="tanh")
def _silu(x):
    return torch.nn.functional.silu(x)
def _sigmoid(x):
    return torch.sigmoid(x)
def _layernorm(x, axis=-1, eps=1e-5):
    m = x.mean(dim=axis, keepdim=True)
    v = x.var(dim=axis, keepdim=True, unbiased=False)
    return (x - m) / torch.sqrt(v + eps)
def _scatter_add(tgt, idx, src, axis=0):
    return tgt.clone().index_add_(axis, idx, src)
def _segment_sum(src, seg, n):
    out = torch.zeros((n,) + src.shape[1:], dtype=src.dtype)
    return out.index_add_(0, seg, src)
def _pad(x, pw):
    flat = tuple(v for pair in reversed(pw) for v in pair)
    return torch.nn.functional.pad(x, flat)

kernel_name = 'parallel_sum__ssm_conv_h3__swiglu_ffn__prompt'

def setup_inputs():

    _w = (lambda a: (lambda t: t.to(torch.bfloat16) if t.is_floating_point() else t)(torch.as_tensor(a)))
    def _A_setup():
        B, S, D = 1, 2048, 512
        ks = 4
        return {
            "q": _w((lambda *s: torch.randn(*s, dtype=torch.bfloat16) * 0.05)(B, S, D)),
            "k": _w((lambda *s: torch.randn(*s, dtype=torch.bfloat16) * 0.4)(B, S, D)),
            "v": _w((lambda *s: torch.randn(*s, dtype=torch.bfloat16) * 0.4)(B, S, D)),
            "shift_k": _w((lambda *s: torch.randn(*s, dtype=torch.bfloat16) * 0.4)(D, ks) * 0.5),
            "ssm_k": _w((lambda *s: torch.randn(*s, dtype=torch.bfloat16) * 1.6)(D, S) * (1.0 / S ** 0.5)),
            "ssm_D": _w((lambda *s: torch.randn(*s, dtype=torch.bfloat16) * 0.8)(D)),
        }
    _A_in = _A_setup()

    def _B_setup():
        B, S, D, F = 1, 2048, 512, 1376
        return {
            "x": _w((lambda *s: torch.randn(*s, dtype=torch.bfloat16) * 0.05)(B, S, D)),
            "w_gate": _w((lambda *s: torch.randn(*s, dtype=torch.bfloat16) * 0.05)(D, F)),
            "w_up": _w((lambda *s: torch.randn(*s, dtype=torch.bfloat16) * 0.05)(D, F)),
            "w_down": _w((lambda *s: torch.randn(*s, dtype=torch.bfloat16) * 0.05)(F, D)),
        }
    _B_in = _B_setup()

    return {
        "A_q": _A_in["q"],
        "A_k": _A_in["k"],
        "A_v": _A_in["v"],
        "A_shift_k": _A_in["shift_k"],
        "A_ssm_k": _A_in["ssm_k"],
        "A_ssm_D": _A_in["ssm_D"],
        "B_x": _B_in["x"],
        "B_w_gate": _B_in["w_gate"],
        "B_w_up": _B_in["w_up"],
        "B_w_down": _B_in["w_down"],
    }

def reference(A_q, A_k, A_v, A_shift_k, A_ssm_k, A_ssm_D, B_x, B_w_gate, B_w_up, B_w_down):
    def _A_ref(q, k, v, shift_k, ssm_k, ssm_D):
        B, S, D = q.shape
        ks = shift_k.shape[-1]
        k_pad = _pad(k, ((0, 0), (ks - 1, 0), (0, 0)))
        k_shift = torch.zeros_like(k)
        for t in range(ks):
            k_shift = k_shift + k_pad[:, t:t + S, :] * shift_k[:, ks - 1 - t][None, None, :]
        kv = k_shift * v
        L = 2 * S
        kv_pad = _pad(kv, ((0, 0), (0, S), (0, 0)))
        kf_pad = _pad(ssm_k, ((0, 0), (0, S)))
        kv_f = (lambda x, axes=(-2,-1): torch.fft.rfft2(x.float(), dim=axes))(kv_pad[:, None, :, :], axes=(1, 2))
        kf_f = (lambda x, axes=(-2,-1): torch.fft.rfft2(x.float(), dim=axes))(kf_pad[:, None, :], axes=(1, 2))
        kf_f = (lambda x, *d: x.permute(*d))(kf_f, 1, 2, 0)
        y_f = kv_f * kf_f[None, :, :, :]
        y = (lambda x, s, axes=(-2,-1): torch.fft.irfft2(x, s=s, dim=axes).to(torch.bfloat16))(y_f, (1, L), axes=(1, 2))[:, 0, :S, :]
        y = y + kv * ssm_D[None, None, :]
        return q * y

    def _B_ref(x, w_gate, w_up, w_down):
        g = _silu(x @ w_gate)
        u = x @ w_up
        return (g * u) @ w_down

    x = A_q
    xn = _layernorm(x, axis=-1)
    return x + _A_ref(xn, A_k, A_v, A_shift_k, A_ssm_k, A_ssm_D) + _B_ref(xn, B_w_gate, B_w_up, B_w_down)
```

```cpp
#include <hip/hip_runtime.h>
#include <hip/hip_bf16.h>

typedef unsigned short u16;
__device__ __forceinline__ float bf2f(u16 v){ return __uint_as_float(((unsigned)v)<<16); }
__device__ __forceinline__ u16 f2bf(float f){ __hip_bfloat16 b = __float2bfloat16(f); return *reinterpret_cast<u16*>(&b); }
__device__ __forceinline__ float rbf(float f){ return bf2f(f2bf(f)); }

#define S_ 2048
#define D_ 512
#define F_ 1376

__global__ void k_ln(const u16* q, u16* xn){
  int row = blockIdx.x; int t = threadIdx.x;
  float v[8]; float s=0;
  for(int j=0;j<8;j++){ v[j]=bf2f(q[row*D_+t*8+j]); s+=v[j]; }
  for(int o=32;o>0;o>>=1) s+=__shfl_xor(s,o);
  float mean=s/512.f; float ss=0;
  for(int j=0;j<8;j++){ float d=v[j]-mean; ss+=d*d; }
  for(int o=32;o>0;o>>=1) ss+=__shfl_xor(ss,o);
  float var=ss/512.f;
  float mb=rbf(mean), vb=rbf(var);
  float den=rbf(sqrtf(rbf(vb+1e-5f)));
  for(int j=0;j<8;j++) xn[row*D_+t*8+j]=f2bf(rbf(v[j]-mb)/den);
}
__global__ void k_kv(const u16* k, const u16* v, const u16* sk, u16* kv){
  int i = blockIdx.x*256+threadIdx.x; int s=i/D_, d=i%D_;
  float acc=0;
  for(int t=0;t<4;t++){
    int r=s-3+t; float kk = r>=0? bf2f(k[r*D_+d]):0.f;
    float p=rbf(kk*bf2f(sk[d*4+3-t]));
    acc=rbf(acc+p);
  }
  kv[i]=f2bf(acc*bf2f(v[i]));
}
__global__ void k_conv(const u16* kv, const u16* h, const u16* Dv, u16* yf){
  int i = blockIdx.x*256+threadIdx.x; int s=i/D_, d=i%D_;
  float acc=0;
  for(int r=0;r<=s;r++) acc+=bf2f(h[d*S_+(s-r)])*bf2f(kv[r*D_+d]);
  float y=rbf(acc);
  float kd=rbf(bf2f(kv[i])*bf2f(Dv[d]));
  yf[i]=f2bf(y+kd);
}
__global__ void k_g1(const u16* xn, const u16* wg, const u16* wu, u16* hh){
  int i = blockIdx.x*256+threadIdx.x; int s=i/F_, f=i%F_;
  if(s>=S_) return;
  float g=0,u=0;
  for(int k=0;k<D_;k++){ float a=bf2f(xn[s*D_+k]); g+=a*bf2f(wg[k*F_+f]); u+=a*bf2f(wu[k*F_+f]); }
  g=rbf(g); u=rbf(u);
  float sg=rbf(g/(1.f+__expf(-g)));
  hh[i]=f2bf(sg*u);
}
__global__ void k_g2(const u16* hh, const u16* wd, const u16* q, const u16* xn, const u16* yf, u16* out){
  int i = blockIdx.x*256+threadIdx.x; int s=i/D_, d=i%D_;
  float acc=0;
  for(int k=0;k<F_;k++) acc+=bf2f(hh[s*F_+k])*bf2f(wd[k*D_+d]);
  float b=rbf(acc);
  float a=rbf(bf2f(xn[i])*bf2f(yf[i]));
  float r1=rbf(bf2f(q[i])+a);
  out[i]=f2bf(r1+b);
}
extern "C" void kernel_launch(void* const* d_in, const int* in_sizes, int n_in,
                              void* d_out, int out_size, void* d_ws, size_t ws_size,
                              hipStream_t stream) {
  const u16* q=(const u16*)d_in[0]; const u16* k=(const u16*)d_in[1]; const u16* v=(const u16*)d_in[2];
  const u16* sk=(const u16*)d_in[3]; const u16* h=(const u16*)d_in[4]; const u16* Dv=(const u16*)d_in[5];
  const u16* wg=(const u16*)d_in[7]; const u16* wu=(const u16*)d_in[8]; const u16* wd=(const u16*)d_in[9];
  u16* ws=(u16*)d_ws;
  u16* xn=ws; u16* kv=ws+S_*D_; u16* yf=ws+2*S_*D_; u16* hh=ws+3*S_*D_;
  k_ln<<<S_,64,0,stream>>>(q,xn);
  k_kv<<<S_*D_/256,256,0,stream>>>(k,v,sk,kv);
  k_conv<<<S_*D_/256,256,0,stream>>>(kv,h,Dv,yf);
  k_g1<<<S_*F_/256,256,0,stream>>>(xn,wg,wu,hh);
  k_g2<<<S_*D_/256,256,0,stream>>>(hh,wd,q,xn,yf,(u16*)d_out);
}
```

```cpp
#include <hip/hip_runtime.h>
#include <hip/hip_bf16.h>

typedef unsigned short u16;
typedef __attribute__((ext_vector_type(8))) short bf16x8;
typedef __attribute__((ext_vector_type(4))) short s16x4;
typedef __attribute__((ext_vector_type(16))) float f32x16;
typedef __attribute__((ext_vector_type(4))) unsigned u32x4;
typedef __attribute__((ext_vector_type(2))) unsigned u32x2;
#define LDSP(T) __attribute__((address_space(3))) T*

#define S_ 2048
#define D_ 512
#define F_ 1376

__device__ __forceinline__ float bf2f(u16 v){ return __uint_as_float(((unsigned)v)<<16); }
__device__ __forceinline__ u16 f2bf(float f){ __hip_bfloat16 b = __float2bfloat16(f); return *reinterpret_cast<u16*>(&b); }
__device__ __forceinline__ float rbf(float f){ return bf2f(f2bf(f)); }

template<int NROWS>
__device__ __forceinline__ void ln_stats(const u16* __restrict__ q, int row0, float* st  ){
  const int tid=threadIdx.x, sub=tid&15, rgrp=tid>>4;
  #pragma unroll
  for(int it=0; it<NROWS/16; ++it){
    const int r = rgrp + it*16;
    const u16* p = q + (size_t)(row0+r)*D_ + sub*8;
    u32x4 v[4];
    #pragma unroll
    for(int j=0;j<4;j++) v[j]=*(const u32x4*)(p+128*j);
    float f[32]; float s=0.f;
    #pragma unroll
    for(int j=0;j<4;j++){
      #pragma unroll
      for(int e=0;e<4;e++){ unsigned w=v[j][e]; f[j*8+e*2]=__uint_as_float(w<<16); f[j*8+e*2+1]=__uint_as_float(w&0xffff0000u); s+=f[j*8+e*2]+f[j*8+e*2+1]; }
    }
    #pragma unroll
    for(int o=8;o>0;o>>=1) s+=__shfl_xor(s,o);
    const float mean=s*(1.f/512.f); float ss=0.f;
    #pragma unroll
    for(int j=0;j<32;j++){ float d=f[j]-mean; ss+=d*d; }
    #pragma unroll
    for(int o=8;o>0;o>>=1) ss+=__shfl_xor(ss,o);
    const float var=ss*(1.f/512.f);
    if(sub==0){ st[r*2]=rbf(mean); st[r*2+1]=rbf(sqrtf(rbf(rbf(var)+1e-5f))); }
  }
}

#define HC_CS 4416
#define HC_CH (4*HC_CS)
#define U_ROW 144
#define U_CH  (33*U_ROW)
#define U_BASE (2*HC_CH)
#define G1_AROW 144
#define G1_ABUF (128*G1_AROW)
#define G1_BROW 320
#define G1_BBUF (64*G1_BROW)
#define G1_STAT (2*(G1_ABUF+G1_BBUF))
#define K1_LDS (G1_STAT+1024)

__device__ __forceinline__ void conv_wg(char* smem, int ci, const u16* __restrict__ k, const u16* __restrict__ v,
    const u16* __restrict__ sk, const u16* __restrict__ h, const u16* __restrict__ Dv, u16* __restrict__ yT){
  const int tid=threadIdx.x, lane=tid&63, wv=tid>>6;
  const int d0=2*ci;
  {
    unsigned kk[11], vv[8];
    #pragma unroll
    for(int j=0;j<11;j++){ int r=8*tid-3+j; kk[j] = (r>=0)? *(const unsigned*)(k+(size_t)r*D_+d0) : 0u; }
    #pragma unroll
    for(int j=0;j<8;j++) vv[j]=*(const unsigned*)(v+(size_t)(8*tid+j)*D_+d0);
    float w[2][4];
    #pragma unroll
    for(int c=0;c<2;c++){ u32x2 t=*(const u32x2*)(sk+(d0+c)*4); w[c][0]=__uint_as_float(t[0]<<16); w[c][1]=__uint_as_float(t[0]&0xffff0000u); w[c][2]=__uint_as_float(t[1]<<16); w[c][3]=__uint_as_float(t[1]&0xffff0000u); }
    #pragma unroll
    for(int c=0;c<2;c++){
      u16 o[8];
      #pragma unroll
      for(int j=0;j<8;j++){
        float acc=0.f;
        #pragma unroll
        for(int t=0;t<4;t++){ unsigned x=kk[j+t]; float kf = c? __uint_as_float(x&0xffff0000u):__uint_as_float(x<<16); acc=rbf(acc+rbf(kf*w[c][3-t])); }
        float vf = c? __uint_as_float(vv[j]&0xffff0000u):__uint_as_float(vv[j]<<16);
        o[j]=f2bf(acc*vf);
      }
      u32x4 pk; pk[0]=o[0]|((unsigned)o[1]<<16); pk[1]=o[2]|((unsigned)o[3]<<16); pk[2]=o[4]|((unsigned)o[5]<<16); pk[3]=o[6]|((unsigned)o[7]<<16);
      *(u32x4*)(smem+U_BASE+c*U_CH+(1+(tid>>3))*U_ROW+(tid&7)*16)=pk;
    }
    if(tid<18){ int c=tid/9, j=tid%9; u32x4 z={0,0,0,0}; *(u32x4*)(smem+U_BASE+c*U_CH+j*16)=z; }
  }
  for(int idx=tid; idx<2*264; idx+=256){
    const int c=idx/264, t=idx%264;
    u16 g[16];
    #pragma unroll
    for(int half=0; half<2; half++){
      int i0=8*t+8*half;
      if(i0<2048){ u32x4 x=*(const u32x4*)(h+(size_t)(d0+c)*S_+(2040-i0));
        #pragma unroll
        for(int e=0;e<4;e++){ g[half*8+7-2*e]=(u16)(x[e]&0xffff); g[half*8+6-2*e]=(u16)(x[e]>>16); } }
      else {
        #pragma unroll
        for(int e=0;e<8;e++) g[half*8+e]=0; }
    }
    #pragma unroll
    for(int cc=0;cc<4;cc++){
      u32x4 pk;
      #pragma unroll
      for(int e=0;e<4;e++) pk[e]=g[cc+2*e]|((unsigned)g[cc+2*e+1]<<16);
      *(u32x4*)(smem+c*HC_CH+cc*HC_CS+t*16)=pk;
    }
  }
  __syncthreads();
  const int ch=wv>>1, half=wv&1, r=lane&31, hh=lane>>5;
  f32x16 acc0={}, acc1={};
  const char* ub = smem+U_BASE+ch*U_CH+hh*16;
  auto ldA=[&](int f)->bf16x8{
    int s0=2047-16*f-r+8*hh; s0=s0<0?0:s0; int c=s0&3;
    const char* p=smem+ch*HC_CH+c*HC_CS+(s0-c)*2;
    u32x2 a=*(const u32x2*)p, b=*(const u32x2*)(p+8);
    u32x4 t={a[0],a[1],b[0],b[1]}; return __builtin_bit_cast(bf16x8,t);
  };
  const int dl0=half*16;
  bf16x8 a_m1=ldA(4*dl0-1), a_m2=ldA(4*dl0-2), a_m3=ldA(4*dl0-3);
  #pragma unroll 2
  for(int dl=dl0; dl<dl0+16; ++dl){
    int rowi=r-dl+1; rowi = rowi<0?0:rowi;
    const char* bp=ub+rowi*U_ROW;
    bf16x8 b0=*(const bf16x8*)(bp), b1=*(const bf16x8*)(bp+32), b2=*(const bf16x8*)(bp+64), b3=*(const bf16x8*)(bp+96);
    bf16x8 a0=ldA(4*dl), a1=ldA(4*dl+1), a2=ldA(4*dl+2);
    acc0=__builtin_amdgcn_mfma_f32_32x32x16_bf16(a0,  b0,acc0,0,0,0);
    acc1=__builtin_amdgcn_mfma_f32_32x32x16_bf16(a2,  b0,acc1,0,0,0);
    acc0=__builtin_amdgcn_mfma_f32_32x32x16_bf16(a_m1,b1,acc0,0,0,0);
    acc1=__builtin_amdgcn_mfma_f32_32x32x16_bf16(a1,  b1,acc1,0,0,0);
    acc0=__builtin_amdgcn_mfma_f32_32x32x16_bf16(a_m2,b2,acc0,0,0,0);
    acc1=__builtin_amdgcn_mfma_f32_32x32x16_bf16(a0,  b2,acc1,0,0,0);
    acc0=__builtin_amdgcn_mfma_f32_32x32x16_bf16(a_m3,b3,acc0,0,0,0);
    acc1=__builtin_amdgcn_mfma_f32_32x32x16_bf16(a_m1,b3,acc1,0,0,0);
    a_m1=ldA(4*dl+3); a_m2=a2; a_m3=a1;
  }
  __syncthreads();
  float* red=(float*)(smem+ch*HC_CH);
  {
    f32x16 snd = half? acc0:acc1;
    #pragma unroll
    for(int i=0;i<16;i++) red[(half*16+i)*64+lane]=snd[i];
  }
  __syncthreads();
  f32x16 acc = half? acc1:acc0;
  #pragma unroll
  for(int i=0;i<16;i++) acc[i]+=red[((1-half)*16+i)*64+lane];
  const float dval=bf2f(Dv[d0+ch]);
  const char* urow=smem+U_BASE+ch*U_CH+(1+r)*U_ROW;
  #pragma unroll
  for(int g4=0; g4<4; ++g4){
    const int b=half*32+8*g4+4*hh;
    u32x2 kvp=*(const u32x2*)(urow+b*2);
    u16 o[4];
    #pragma unroll
    for(int e=0;e<4;e++){
      unsigned x=kvp[e>>1]; float kvf=(e&1)? __uint_as_float(x&0xffff0000u):__uint_as_float(x<<16);
      o[e]=f2bf(rbf(acc[g4*4+e])+rbf(kvf*dval));
    }
    u32x2 pk; pk[0]=o[0]|((unsigned)o[1]<<16); pk[1]=o[2]|((unsigned)o[3]<<16);
    *(u32x2*)(yT+(size_t)(d0+ch)*S_+64*r+b)=pk;
  }
}

__device__ __forceinline__ void gemm1_wg(char* smem, int gi, const u16* __restrict__ q, const u16* __restrict__ wg,
    const u16* __restrict__ wu, u16* __restrict__ hbuf){
  const int tid=threadIdx.x, lane=tid&63, wv=tid>>6;
  const int rt=gi/22, ct=gi%22, row0=rt*128, f0=ct*64;
  float* st=(float*)(smem+G1_STAT);
  ln_stats<128>(q,row0,st);
  __syncthreads();
  u32x4 ra[4], rb[4];
  auto gload=[&](int ks){
    #pragma unroll
    for(int i=0;i<4;i++){ int id=tid+256*i, row=id>>3, c8=id&7; ra[i]=*(const u32x4*)(q+(size_t)(row0+row)*D_+ks*64+c8*8); }
    #pragma unroll
    for(int i=0;i<4;i++){ int id=tid+256*i, kr=id>>4, c16=id&15; int f=f0+(c16&7)*8; if(f>F_-8) f=F_-8;
      const u16* src=(c16<8)? wg:wu; rb[i]=*(const u32x4*)(src+(size_t)(ks*64+kr)*F_+f); }
  };
  auto lstore=[&](int buf){
    char* A=smem+buf*(G1_ABUF+G1_BBUF); char* B=A+G1_ABUF;
    #pragma unroll
    for(int i=0;i<4;i++){ int id=tid+256*i, row=id>>3, c8=id&7; float mb=st[row*2], den=st[row*2+1]; u32x4 o;
      #pragma unroll
      for(int e=0;e<4;e++){ unsigned x=ra[i][e]; float lo=__uint_as_float(x<<16), hi=__uint_as_float(x&0xffff0000u);
        o[e]=f2bf(rbf(lo-mb)/den)|((unsigned)f2bf(rbf(hi-mb)/den)<<16); }
      *(u32x4*)(A+row*G1_AROW+c8*16)=o; }
    #pragma unroll
    for(int i=0;i<4;i++){ int id=tid+256*i, kr=id>>4, c16=id&15; *(u32x4*)(B+kr*G1_BROW+c16*16)=rb[i]; }
  };
  const int wr=wv>>1, wc=wv&1, r=lane&31, hh=lane>>5;
  const int g=lane>>4, bq=(lane&15)>>2, bp=lane&3;
  f32x16 ag[2]={}, au[2]={};
  gload(0); lstore(0); __syncthreads();
  for(int ks=0; ks<8; ++ks){
    if(ks<7) gload(ks+1);
    const char* A=smem+(ks&1)*(G1_ABUF+G1_BBUF); const char* B=A+G1_ABUF;
    #pragma unroll
    for(int k4=0;k4<4;k4++){
      bf16x8 a0=*(const bf16x8*)(A+(wr*64+r)*G1_AROW+(k4*2+hh)*16);
      bf16x8 a1=*(const bf16x8*)(A+(wr*64+32+r)*G1_AROW+(k4*2+hh)*16);
      const char* bb=B+(16*k4+8*(g>>1)+bq)*G1_BROW+(wc*32+16*(g&1)+4*bp)*2;
      s16x4 g0=__builtin_amdgcn_ds_read_tr16_b64_v4i16((LDSP(s16x4))(bb));
      s16x4 g1=__builtin_amdgcn_ds_read_tr16_b64_v4i16((LDSP(s16x4))(bb+4*G1_BROW));
      s16x4 u0=__builtin_amdgcn_ds_read_tr16_b64_v4i16((LDSP(s16x4))(bb+128));
      s16x4 u1=__builtin_amdgcn_ds_read_tr16_b64_v4i16((LDSP(s16x4))(bb+128+4*G1_BROW));
      bf16x8 bg={g0[0],g0[1],g0[2],g0[3],g1[0],g1[1],g1[2],g1[3]};
      bf16x8 bu={u0[0],u0[1],u0[2],u0[3],u1[0],u1[1],u1[2],u1[3]};
      ag[0]=__builtin_amdgcn_mfma_f32_32x32x16_bf16(a0,bg,ag[0],0,0,0);
      au[0]=__builtin_amdgcn_mfma_f32_32x32x16_bf16(a0,bu,au[0],0,0,0);
      ag[1]=__builtin_amdgcn_mfma_f32_32x32x16_bf16(a1,bg,ag[1],0,0,0);
      au[1]=__builtin_amdgcn_mfma_f32_32x32x16_bf16(a1,bu,au[1],0,0,0);
    }
    if(ks<7){ lstore((ks+1)&1); }
    __syncthreads();
  }
  const int f=f0+wc*32+r;
  if(f<F_){
    #pragma unroll
    for(int m=0;m<2;m++){
      #pragma unroll
      for(int i=0;i<16;i++){
        int row=row0+wr*64+m*32+(i&3)+8*(i>>2)+4*hh;
        float gg=rbf(ag[m][i]), uu=rbf(au[m][i]);
        float sg=rbf(gg/(1.f+__expf(-gg)));
        hbuf[(size_t)row*F_+f]=f2bf(sg*uu);
      }
    }
  }
}

__global__ __launch_bounds__(256,2) void k1(const u16* __restrict__ q, const u16* __restrict__ k, const u16* __restrict__ v,
    const u16* __restrict__ sk, const u16* __restrict__ h, const u16* __restrict__ Dv,
    const u16* __restrict__ wg, const u16* __restrict__ wu, u16* __restrict__ yT, u16* __restrict__ hbuf){
  extern __shared__ __attribute__((aligned(16))) char smem[];
  const int bid=blockIdx.x;
  if(bid<256){ conv_wg(smem,(bid&7)*32+(bid>>3),k,v,sk,h,Dv,yT); }
  else { gemm1_wg(smem,bid-256,q,wg,wu,hbuf); }
}

#define G2_AROW 144
#define G2_ABUF (64*G2_AROW)
#define G2_BROW 192
#define G2_BBUF (64*G2_BROW)
#define G2_STAT (2*(G2_ABUF+G2_BBUF))
#define K2_LDS (G2_STAT+512)
__global__ __launch_bounds__(256,2) void k2(const u16* __restrict__ q, const u16* __restrict__ hbuf, const u16* __restrict__ wd,
    const u16* __restrict__ yT, u16* __restrict__ out){
  extern __shared__ __attribute__((aligned(16))) char smem[];
  const int tid=threadIdx.x, lane=tid&63, wv=tid>>6;
  const int rt=blockIdx.x>>3, ct=blockIdx.x&7, row0=rt*64, d0=ct*64;
  float* st=(float*)(smem+G2_STAT);
  u32x4 ra[2], rb[2];
  auto gload=[&](int ks){
    #pragma unroll
    for(int i=0;i<2;i++){ int id=tid+256*i, row=id>>3, c8=id&7; int kk=ks*64+c8*8; u32x4 z={0,0,0,0};
      ra[i]= kk<F_? *(const u32x4*)(hbuf+(size_t)(row0+row)*F_+kk) : z; }
    #pragma unroll
    for(int i=0;i<2;i++){ int id=tid+256*i, kr=id>>3, c8=id&7; int kk=ks*64+kr; u32x4 z={0,0,0,0};
      rb[i]= kk<F_? *(const u32x4*)(wd+(size_t)kk*D_+d0+c8*8) : z; }
  };
  auto lstore=[&](int buf){
    char* A=smem+buf*(G2_ABUF+G2_BBUF); char* B=A+G2_ABUF;
    #pragma unroll
    for(int i=0;i<2;i++){ int id=tid+256*i, row=id>>3, c8=id&7; *(u32x4*)(A+row*G2_AROW+c8*16)=ra[i]; }
    #pragma unroll
    for(int i=0;i<2;i++){ int id=tid+256*i, kr=id>>3, c8=id&7; *(u32x4*)(B+kr*G2_BROW+c8*16)=rb[i]; }
  };
  gload(0);
  ln_stats<64>(q,row0,st);
  lstore(0); __syncthreads();
  const int wr=wv>>1, wc=wv&1, r=lane&31, hh=lane>>5;
  const int g=lane>>4, bq=(lane&15)>>2, bp=lane&3;
  f32x16 acc={};
  for(int ks=0; ks<22; ++ks){
    if(ks<21) gload(ks+1);
    const char* A=smem+(ks&1)*(G2_ABUF+G2_BBUF); const char* B=A+G2_ABUF;
    #pragma unroll
    for(int k4=0;k4<4;k4++){
      bf16x8 a0=*(const bf16x8*)(A+(wr*32+r)*G2_AROW+(k4*2+hh)*16);
      const char* bb=B+(16*k4+8*(g>>1)+bq)*G2_BROW+(wc*32+16*(g&1)+4*bp)*2;
      s16x4 b0=__builtin_amdgcn_ds_read_tr16_b64_v4i16((LDSP(s16x4))(bb));
      s16x4 b1=__builtin_amdgcn_ds_read_tr16_b64_v4i16((LDSP(s16x4))(bb+4*G2_BROW));
      bf16x8 bf={b0[0],b0[1],b0[2],b0[3],b1[0],b1[1],b1[2],b1[3]};
      acc=__builtin_amdgcn_mfma_f32_32x32x16_bf16(a0,bf,acc,0,0,0);
    }
    if(ks<21) lstore((ks+1)&1);
    __syncthreads();
  }
  const int d=d0+wc*32+r;
  #pragma unroll
  for(int g4=0; g4<4; ++g4){
    const int lr=wr*32+8*g4+4*hh;
    u32x2 yp=*(const u32x2*)(yT+(size_t)d*S_+row0+lr);
    #pragma unroll
    for(int e=0;e<4;e++){
      const int row=row0+lr+e;
      float x=bf2f(q[(size_t)row*D_+d]);
      float xn=rbf(rbf(x-st[(lr+e)*2])/st[(lr+e)*2+1]);
      unsigned yy=yp[e>>1]; float yf=(e&1)? __uint_as_float(yy&0xffff0000u):__uint_as_float(yy<<16);
      float r1=rbf(x+rbf(xn*yf));
      out[(size_t)row*D_+d]=f2bf(r1+rbf(acc[g4*4+e]));
    }
  }
}

extern "C" void kernel_launch(void* const* d_in, const int* in_sizes, int n_in,
                              void* d_out, int out_size, void* d_ws, size_t ws_size,
                              hipStream_t stream) {
  const u16* q=(const u16*)d_in[0]; const u16* k=(const u16*)d_in[1]; const u16* v=(const u16*)d_in[2];
  const u16* sk=(const u16*)d_in[3]; const u16* h=(const u16*)d_in[4]; const u16* Dv=(const u16*)d_in[5];
  const u16* wg=(const u16*)d_in[7]; const u16* wu=(const u16*)d_in[8]; const u16* wd=(const u16*)d_in[9];
  u16* yT=(u16*)d_ws; u16* hbuf=yT+(size_t)S_*D_;
  (void)hipFuncSetAttribute((const void*)k1, hipFuncAttributeMaxDynamicSharedMemorySize, K1_LDS);
  k1<<<256+352,256,K1_LDS,stream>>>(q,k,v,sk,h,Dv,wg,wu,yT,hbuf);
  k2<<<256,256,K2_LDS,stream>>>(q,hbuf,wd,yT,(u16*)d_out);
}
```

```cpp
#include <hip/hip_runtime.h>
#include <hip/hip_bf16.h>

typedef unsigned short u16;
typedef __attribute__((ext_vector_type(8))) short bf16x8;
typedef __attribute__((ext_vector_type(4))) short s16x4;
typedef __attribute__((ext_vector_type(16))) float f32x16;
typedef __attribute__((ext_vector_type(4))) unsigned u32x4;
typedef __attribute__((ext_vector_type(2))) unsigned u32x2;
#define LDSP(T) __attribute__((address_space(3))) T*

#define S_ 2048
#define D_ 512
#define F_ 1376

__device__ __forceinline__ float bf2f(u16 v){ return __uint_as_float(((unsigned)v)<<16); }
__device__ __forceinline__ u16 f2bf(float f){ __hip_bfloat16 b = __float2bfloat16(f); return *reinterpret_cast<u16*>(&b); }
__device__ __forceinline__ float rbf(float f){ return bf2f(f2bf(f)); }

template<int NROWS>
__device__ __forceinline__ void ln_stats(const u16* __restrict__ q, int row0, float* st  ){
  const int tid=threadIdx.x, sub=tid&15, rgrp=tid>>4;
  #pragma unroll
  for(int it=0; it<NROWS/16; ++it){
    const int r = rgrp + it*16;
    const u16* p = q + (size_t)(row0+r)*D_ + sub*8;
    u32x4 v[4];
    #pragma unroll
    for(int j=0;j<4;j++) v[j]=*(const u32x4*)(p+128*j);
    float f[32]; float s=0.f;
    #pragma unroll
    for(int j=0;j<4;j++){
      #pragma unroll
      for(int e=0;e<4;e++){ unsigned w=v[j][e]; f[j*8+e*2]=__uint_as_float(w<<16); f[j*8+e*2+1]=__uint_as_float(w&0xffff0000u); s+=f[j*8+e*2]+f[j*8+e*2+1]; }
    }
    #pragma unroll
    for(int o=8;o>0;o>>=1) s+=__shfl_xor(s,o);
    const float mean=s*(1.f/512.f); float ss=0.f;
    #pragma unroll
    for(int j=0;j<32;j++){ float d=f[j]-mean; ss+=d*d; }
    #pragma unroll
    for(int o=8;o>0;o>>=1) ss+=__shfl_xor(ss,o);
    const float var=ss*(1.f/512.f);
    if(sub==0){ st[r*2]=rbf(mean); st[r*2+1]=1.f/rbf(sqrtf(rbf(rbf(var)+1e-5f))); }
  }
}

#define HC_CS 4416
#define HC_CH (4*HC_CS)
#define U_ROW 144
#define U_CH  (33*U_ROW)
#define U_BASE (2*HC_CH)
#define G1_AROW 144
#define G1_ABUF (128*G1_AROW)
#define G1_BROW 320
#define G1_BBUF (64*G1_BROW)
#define G1_STAT (2*(G1_ABUF+G1_BBUF))
#define K1_LDS (G1_STAT+1024)

__device__ __forceinline__ void conv_wg(char* smem, int ci, const u16* __restrict__ k, const u16* __restrict__ v,
    const u16* __restrict__ sk, const u16* __restrict__ h, const u16* __restrict__ Dv, u16* __restrict__ yT){
  const int tid=threadIdx.x, lane=tid&63, wv=tid>>6;
  const int d0=2*ci;
  {
    unsigned kk[11], vv[8];
    #pragma unroll
    for(int j=0;j<11;j++){ int r=8*tid-3+j; kk[j] = (r>=0)? *(const unsigned*)(k+(size_t)r*D_+d0) : 0u; }
    #pragma unroll
    for(int j=0;j<8;j++) vv[j]=*(const unsigned*)(v+(size_t)(8*tid+j)*D_+d0);
    float w[2][4];
    #pragma unroll
    for(int c=0;c<2;c++){ u32x2 t=*(const u32x2*)(sk+(d0+c)*4); w[c][0]=__uint_as_float(t[0]<<16); w[c][1]=__uint_as_float(t[0]&0xffff0000u); w[c][2]=__uint_as_float(t[1]<<16); w[c][3]=__uint_as_float(t[1]&0xffff0000u); }
    #pragma unroll
    for(int c=0;c<2;c++){
      u16 o[8];
      #pragma unroll
      for(int j=0;j<8;j++){
        float acc=0.f;
        #pragma unroll
        for(int t=0;t<4;t++){ unsigned x=kk[j+t]; float kf = c? __uint_as_float(x&0xffff0000u):__uint_as_float(x<<16); acc=rbf(acc+rbf(kf*w[c][3-t])); }
        float vf = c? __uint_as_float(vv[j]&0xffff0000u):__uint_as_float(vv[j]<<16);
        o[j]=f2bf(acc*vf);
      }
      u32x4 pk; pk[0]=o[0]|((unsigned)o[1]<<16); pk[1]=o[2]|((unsigned)o[3]<<16); pk[2]=o[4]|((unsigned)o[5]<<16); pk[3]=o[6]|((unsigned)o[7]<<16);
      *(u32x4*)(smem+U_BASE+c*U_CH+(1+(tid>>3))*U_ROW+(tid&7)*16)=pk;
    }
    if(tid<18){ int c=tid/9, j=tid%9; u32x4 z={0,0,0,0}; *(u32x4*)(smem+U_BASE+c*U_CH+j*16)=z; }
  }
  for(int idx=tid; idx<2*264; idx+=256){
    const int c=idx/264, t=idx%264;
    u16 g[16];
    #pragma unroll
    for(int half=0; half<2; half++){
      int i0=8*t+8*half;
      if(i0<2048){ u32x4 x=*(const u32x4*)(h+(size_t)(d0+c)*S_+(2040-i0));
        #pragma unroll
        for(int e=0;e<4;e++){ g[half*8+7-2*e]=(u16)(x[e]&0xffff); g[half*8+6-2*e]=(u16)(x[e]>>16); } }
      else {
        #pragma unroll
        for(int e=0;e<8;e++) g[half*8+e]=0; }
    }
    #pragma unroll
    for(int cc=0;cc<4;cc++){
      u32x4 pk;
      #pragma unroll
      for(int e=0;e<4;e++) pk[e]=g[cc+2*e]|((unsigned)g[cc+2*e+1]<<16);
      *(u32x4*)(smem+c*HC_CH+cc*HC_CS+t*16)=pk;
    }
  }
  __syncthreads();
  const int ch=wv>>1, half=wv&1, r=lane&31, hh=lane>>5;
  f32x16 acc0={}, acc1={};
  const char* ub = smem+U_BASE+ch*U_CH+hh*16;
  auto ldA=[&](int f)->bf16x8{
    int s0=2047-16*f-r+8*hh; s0=s0<0?0:s0; int c=s0&3;
    const char* p=smem+ch*HC_CH+c*HC_CS+(s0-c)*2;
    u32x2 a=*(const u32x2*)p, b=*(const u32x2*)(p+8);
    u32x4 t={a[0],a[1],b[0],b[1]}; return __builtin_bit_cast(bf16x8,t);
  };
  const int dl0=half*16;
  bf16x8 a_m1=ldA(4*dl0-1), a_m2=ldA(4*dl0-2), a_m3=ldA(4*dl0-3);
  #pragma unroll 2
  for(int dl=dl0; dl<dl0+16; ++dl){
    int rowi=r-dl+1; rowi = rowi<0?0:rowi;
    const char* bp=ub+rowi*U_ROW;
    bf16x8 b0=*(const bf16x8*)(bp), b1=*(const bf16x8*)(bp+32), b2=*(const bf16x8*)(bp+64), b3=*(const bf16x8*)(bp+96);
    bf16x8 a0=ldA(4*dl), a1=ldA(4*dl+1), a2=ldA(4*dl+2);
    acc0=__builtin_amdgcn_mfma_f32_32x32x16_bf16(a0,  b0,acc0,0,0,0);
    acc1=__builtin_amdgcn_mfma_f32_32x32x16_bf16(a2,  b0,acc1,0,0,0);
    acc0=__builtin_amdgcn_mfma_f32_32x32x16_bf16(a_m1,b1,acc0,0,0,0);
    acc1=__builtin_amdgcn_mfma_f32_32x32x16_bf16(a1,  b1,acc1,0,0,0);
    acc0=__builtin_amdgcn_mfma_f32_32x32x16_bf16(a_m2,b2,acc0,0,0,0);
    acc1=__builtin_amdgcn_mfma_f32_32x32x16_bf16(a0,  b2,acc1,0,0,0);
    acc0=__builtin_amdgcn_mfma_f32_32x32x16_bf16(a_m3,b3,acc0,0,0,0);
    acc1=__builtin_amdgcn_mfma_f32_32x32x16_bf16(a_m1,b3,acc1,0,0,0);
    a_m1=ldA(4*dl+3); a_m2=a2; a_m3=a1;
  }
  __syncthreads();
  float* red=(float*)(smem+ch*HC_CH);
  {
    f32x16 snd = half? acc0:acc1;
    #pragma unroll
    for(int i=0;i<16;i++) red[(half*16+i)*64+lane]=snd[i];
  }
  __syncthreads();
  f32x16 acc = half? acc1:acc0;
  #pragma unroll
  for(int i=0;i<16;i++) acc[i]+=red[((1-half)*16+i)*64+lane];
  const float dval=bf2f(Dv[d0+ch]);
  const char* urow=smem+U_BASE+ch*U_CH+(1+r)*U_ROW;
  #pragma unroll
  for(int g4=0; g4<4; ++g4){
    const int b=half*32+8*g4+4*hh;
    u32x2 kvp=*(const u32x2*)(urow+b*2);
    u16 o[4];
    #pragma unroll
    for(int e=0;e<4;e++){
      unsigned x=kvp[e>>1]; float kvf=(e&1)? __uint_as_float(x&0xffff0000u):__uint_as_float(x<<16);
      o[e]=f2bf(rbf(acc[g4*4+e])+rbf(kvf*dval));
    }
    u32x2 pk; pk[0]=o[0]|((unsigned)o[1]<<16); pk[1]=o[2]|((unsigned)o[3]<<16);
    *(u32x2*)(yT+(size_t)(d0+ch)*S_+64*r+b)=pk;
  }
}

__device__ __forceinline__ void gemm1_wg(char* smem, int gi, const u16* __restrict__ q, const u16* __restrict__ wg,
    const u16* __restrict__ wu, u16* __restrict__ hbuf){
  const int tid=threadIdx.x, lane=tid&63, wv=tid>>6;
  const int rt=gi/22, ct=gi%22, row0=rt*128, f0=ct*64;
  float* st=(float*)(smem+G1_STAT);
  u32x4 RA[3][4], RB[3][4];
  auto gload=[&](int ks, u32x4* ra, u32x4* rb){
    #pragma unroll
    for(int i=0;i<4;i++){ int id=tid+256*i, row=id>>3, c8=id&7; ra[i]=*(const u32x4*)(q+(size_t)(row0+row)*D_+ks*64+c8*8); }
    #pragma unroll
    for(int i=0;i<4;i++){ int id=tid+256*i, kr=id>>4, c16=id&15; int f=f0+(c16&7)*8; if(f>F_-8) f=F_-8;
      const u16* src=(c16<8)? wg:wu; rb[i]=*(const u32x4*)(src+(size_t)(ks*64+kr)*F_+f); }
  };
  auto lstore=[&](int buf, const u32x4* ra, const u32x4* rb){
    char* A=smem+buf*(G1_ABUF+G1_BBUF); char* B=A+G1_ABUF;
    #pragma unroll
    for(int i=0;i<4;i++){ int id=tid+256*i, row=id>>3, c8=id&7; float mb=st[row*2], den=st[row*2+1]; u32x4 o;
      #pragma unroll
      for(int e=0;e<4;e++){ unsigned x=ra[i][e]; float lo=__uint_as_float(x<<16), hi=__uint_as_float(x&0xffff0000u);
        o[e]=f2bf(rbf(lo-mb)*den)|((unsigned)f2bf(rbf(hi-mb)*den)<<16); }
      *(u32x4*)(A+row*G1_AROW+c8*16)=o; }
    #pragma unroll
    for(int i=0;i<4;i++){ int id=tid+256*i, kr=id>>4, c16=id&15; *(u32x4*)(B+kr*G1_BROW+c16*16)=rb[i]; }
  };
  const int wr=wv>>1, wc=wv&1, r=lane&31, hh=lane>>5;
  const int g=lane>>4, bq=(lane&15)>>2, bp=lane&3;
  f32x16 ag[2]={}, au[2]={};
  gload(0,RA[0],RB[0]); gload(1,RA[1],RB[1]); gload(2,RA[2],RB[2]);
  ln_stats<128>(q,row0,st);
  __syncthreads();
  lstore(0,RA[0],RB[0]); __syncthreads();
  #pragma unroll
  for(int ks=0; ks<8; ++ks){
    const char* A=smem+(ks&1)*(G1_ABUF+G1_BBUF); const char* B=A+G1_ABUF;
    #pragma unroll
    for(int k4=0;k4<4;k4++){
      bf16x8 a0=*(const bf16x8*)(A+(wr*64+r)*G1_AROW+(k4*2+hh)*16);
      bf16x8 a1=*(const bf16x8*)(A+(wr*64+32+r)*G1_AROW+(k4*2+hh)*16);
      const char* bb=B+(16*k4+8*(g>>1)+bq)*G1_BROW+(wc*32+16*(g&1)+4*bp)*2;
      s16x4 g0=__builtin_amdgcn_ds_read_tr16_b64_v4i16((LDSP(s16x4))(bb));
      s16x4 g1=__builtin_amdgcn_ds_read_tr16_b64_v4i16((LDSP(s16x4))(bb+4*G1_BROW));
      s16x4 u0=__builtin_amdgcn_ds_read_tr16_b64_v4i16((LDSP(s16x4))(bb+128));
      s16x4 u1=__builtin_amdgcn_ds_read_tr16_b64_v4i16((LDSP(s16x4))(bb+128+4*G1_BROW));
      bf16x8 bg={g0[0],g0[1],g0[2],g0[3],g1[0],g1[1],g1[2],g1[3]};
      bf16x8 bu={u0[0],u0[1],u0[2],u0[3],u1[0],u1[1],u1[2],u1[3]};
      ag[0]=__builtin_amdgcn_mfma_f32_32x32x16_bf16(a0,bg,ag[0],0,0,0);
      au[0]=__builtin_amdgcn_mfma_f32_32x32x16_bf16(a0,bu,au[0],0,0,0);
      ag[1]=__builtin_amdgcn_mfma_f32_32x32x16_bf16(a1,bg,ag[1],0,0,0);
      au[1]=__builtin_amdgcn_mfma_f32_32x32x16_bf16(a1,bu,au[1],0,0,0);
    }
    if(ks<7){ lstore((ks+1)&1,RA[(ks+1)%3],RB[(ks+1)%3]); }
    if(ks+3<8) gload(ks+3,RA[ks%3],RB[ks%3]);
    __syncthreads();
  }
  const int f=f0+wc*32+r;
  if(f<F_){
    #pragma unroll
    for(int m=0;m<2;m++){
      #pragma unroll
      for(int i=0;i<16;i++){
        int row=row0+wr*64+m*32+(i&3)+8*(i>>2)+4*hh;
        float gg=rbf(ag[m][i]), uu=rbf(au[m][i]);
        float sg=rbf(gg*__frcp_rn(1.f+__expf(-gg)));
        hbuf[(size_t)row*F_+f]=f2bf(sg*uu);
      }
    }
  }
}

__global__ __launch_bounds__(256,2) void k1(const u16* __restrict__ q, const u16* __restrict__ k, const u16* __restrict__ v,
    const u16* __restrict__ sk, const u16* __restrict__ h, const u16* __restrict__ Dv,
    const u16* __restrict__ wg, const u16* __restrict__ wu, u16* __restrict__ yT, u16* __restrict__ hbuf){
  extern __shared__ __attribute__((aligned(16))) char smem[];
  const int bid=blockIdx.x;
  if(bid<256){ conv_wg(smem,(bid&7)*32+(bid>>3),k,v,sk,h,Dv,yT); }
  else { gemm1_wg(smem,bid-256,q,wg,wu,hbuf); }
}

#define G2_AROW 144
#define G2_ABUF (64*G2_AROW)
#define G2_BROW 192
#define G2_BBUF (64*G2_BROW)
#define G2_STAT (2*(G2_ABUF+G2_BBUF))
#define K2_LDS (G2_STAT+512)
__global__ __launch_bounds__(256,2) void k2(const u16* __restrict__ q, const u16* __restrict__ hbuf, const u16* __restrict__ wd,
    const u16* __restrict__ yT, u16* __restrict__ out){
  extern __shared__ __attribute__((aligned(16))) char smem[];
  const int tid=threadIdx.x, lane=tid&63, wv=tid>>6;
  const int rt=blockIdx.x>>3, ct=blockIdx.x&7, row0=rt*64, d0=ct*64;
  float* st=(float*)(smem+G2_STAT);
  u32x4 RA[4][2], RB[4][2];
  auto gload=[&](int ks, u32x4* ra, u32x4* rb){
    #pragma unroll
    for(int i=0;i<2;i++){ int id=tid+256*i, row=id>>3, c8=id&7; int kk=ks*64+c8*8; u32x4 z={0,0,0,0};
      ra[i]= kk<F_? *(const u32x4*)(hbuf+(size_t)(row0+row)*F_+kk) : z; }
    #pragma unroll
    for(int i=0;i<2;i++){ int id=tid+256*i, kr=id>>3, c8=id&7; int kk=ks*64+kr; u32x4 z={0,0,0,0};
      rb[i]= kk<F_? *(const u32x4*)(wd+(size_t)kk*D_+d0+c8*8) : z; }
  };
  auto lstore=[&](int buf, const u32x4* ra, const u32x4* rb){
    char* A=smem+buf*(G2_ABUF+G2_BBUF); char* B=A+G2_ABUF;
    #pragma unroll
    for(int i=0;i<2;i++){ int id=tid+256*i, row=id>>3, c8=id&7; *(u32x4*)(A+row*G2_AROW+c8*16)=ra[i]; }
    #pragma unroll
    for(int i=0;i<2;i++){ int id=tid+256*i, kr=id>>3, c8=id&7; *(u32x4*)(B+kr*G2_BROW+c8*16)=rb[i]; }
  };
  gload(0,RA[0],RB[0]); gload(1,RA[1],RB[1]); gload(2,RA[2],RB[2]); gload(3,RA[3],RB[3]);
  ln_stats<64>(q,row0,st);
  lstore(0,RA[0],RB[0]); __syncthreads();
  const int wr=wv>>1, wc=wv&1, r=lane&31, hh=lane>>5;
  const int g=lane>>4, bq=(lane&15)>>2, bp=lane&3;
  f32x16 acc={};
  #pragma unroll
  for(int ks=0; ks<22; ++ks){
    const char* A=smem+(ks&1)*(G2_ABUF+G2_BBUF); const char* B=A+G2_ABUF;
    #pragma unroll
    for(int k4=0;k4<4;k4++){
      bf16x8 a0=*(const bf16x8*)(A+(wr*32+r)*G2_AROW+(k4*2+hh)*16);
      const char* bb=B+(16*k4+8*(g>>1)+bq)*G2_BROW+(wc*32+16*(g&1)+4*bp)*2;
      s16x4 b0=__builtin_amdgcn_ds_read_tr16_b64_v4i16((LDSP(s16x4))(bb));
      s16x4 b1=__builtin_amdgcn_ds_read_tr16_b64_v4i16((LDSP(s16x4))(bb+4*G2_BROW));
      bf16x8 bf={b0[0],b0[1],b0[2],b0[3],b1[0],b1[1],b1[2],b1[3]};
      acc=__builtin_amdgcn_mfma_f32_32x32x16_bf16(a0,bf,acc,0,0,0);
    }
    if(ks<21) lstore((ks+1)&1,RA[(ks+1)%4],RB[(ks+1)%4]);
    if(ks+4<22) gload(ks+4,RA[ks%4],RB[ks%4]);
    __syncthreads();
  }
  const int d=d0+wc*32+r;
  #pragma unroll
  for(int g4=0; g4<4; ++g4){
    const int lr=wr*32+8*g4+4*hh;
    u32x2 yp=*(const u32x2*)(yT+(size_t)d*S_+row0+lr);
    #pragma unroll
    for(int e=0;e<4;e++){
      const int row=row0+lr+e;
      float x=bf2f(q[(size_t)row*D_+d]);
      float xn=rbf(rbf(x-st[(lr+e)*2])*st[(lr+e)*2+1]);
      unsigned yy=yp[e>>1]; float yf=(e&1)? __uint_as_float(yy&0xffff0000u):__uint_as_float(yy<<16);
      float r1=rbf(x+rbf(xn*yf));
      out[(size_t)row*D_+d]=f2bf(r1+rbf(acc[g4*4+e]));
    }
  }
}

extern "C" void kernel_launch(void* const* d_in, const int* in_sizes, int n_in,
                              void* d_out, int out_size, void* d_ws, size_t ws_size,
                              hipStream_t stream) {
  const u16* q=(const u16*)d_in[0]; const u16* k=(const u16*)d_in[1]; const u16* v=(const u16*)d_in[2];
  const u16* sk=(const u16*)d_in[3]; const u16* h=(const u16*)d_in[4]; const u16* Dv=(const u16*)d_in[5];
  const u16* wg=(const u16*)d_in[7]; const u16* wu=(const u16*)d_in[8]; const u16* wd=(const u16*)d_in[9];
  u16* yT=(u16*)d_ws; u16* hbuf=yT+(size_t)S_*D_;
  (void)hipFuncSetAttribute((const void*)k1, hipFuncAttributeMaxDynamicSharedMemorySize, K1_LDS);
  k1<<<256+352,256,K1_LDS,stream>>>(q,k,v,sk,h,Dv,wg,wu,yT,hbuf);
  k2<<<256,256,K2_LDS,stream>>>(q,hbuf,wd,yT,(u16*)d_out);
}
```
